```python
import torch

def _softmax(x, axis=-1):
    return torch.softmax(x, dim=axis)
def _gelu(x):
    return torch.nn.functional.gelu(x, approximate="tanh")
def _silu(x):
    return torch.nn.functional.silu(x)
def _sigmoid(x):
    return torch.sigmoid(x)
def _layernorm(x, axis=-1, eps=1e-5):
    m = x.mean(dim=axis, keepdim=True)
    v = x.var(dim=axis, keepdim=True, unbiased=False)
    return (x - m) / torch.sqrt(v + eps)
def _scatter_add(tgt, idx, src, axis=0):
    return tgt.clone().index_add_(axis, idx, src)
def _segment_sum(src, seg, n):
    out = torch.zeros((n,) + src.shape[1:], dtype=src.dtype)
    return out.index_add_(0, seg, src)
def _pad(x, pw):
    flat = tuple(v for pair in reversed(pw) for v in pair)
    return torch.nn.functional.pad(x, flat)

kernel_name = 'parallel_sum__gqa_block_cached__glu_gated_geglu__decode'

def setup_inputs():

    _w = (lambda a: (lambda t: t.to(torch.bfloat16) if t.is_floating_point() else t)(torch.as_tensor(a)))
    def _A_setup():
        B, Sq, D = 8, 1, 1024
        Hq, Hkv, Sk, Dh = 32, 4, 8192, 128
        assert Hq % Hkv == 0, (Hq, Hkv)
        return {
            "x": _w((lambda *s: torch.randn(*s, dtype=torch.bfloat16) * 0.05)(B, Sq, D)),
            "k_cache": _w((lambda *s: torch.randn(*s, dtype=torch.bfloat16) * 1.6)(B, Hkv, Sk, Dh)),
            "v_cache": _w((lambda *s: torch.randn(*s, dtype=torch.bfloat16) * 0.8)(B, Hkv, Sk, Dh)),
            "wq": _w((lambda *s: torch.randn(*s, dtype=torch.bfloat16) * 1.6)(D, Hq * Dh) * (D ** -0.5)),
            "wo": _w((lambda *s: torch.randn(*s, dtype=torch.bfloat16) * 0.8)(Hq * Dh, D) * ((Hq * Dh) ** -0.5)),
        }
    _A_in = _A_setup()

    def _B_setup():
        B, S, Dm, Df = 8, 1, 1024, 3584
        return {
            "x": _w((lambda *s: torch.randn(*s, dtype=torch.bfloat16) * 0.05)(B, S, Dm)),
            "w_gate": _w((lambda *s: torch.randn(*s, dtype=torch.bfloat16) * 0.4)(Dm, Df) * (Dm ** -0.5)),
            "w_up": _w((lambda *s: torch.randn(*s, dtype=torch.bfloat16) * 0.4)(Dm, Df) * (Dm ** -0.5)),
            "w_down": _w((lambda *s: torch.randn(*s, dtype=torch.bfloat16) * 0.4)(Df, Dm) * (Df ** -0.5)),
        }
    _B_in = _B_setup()

    return {
        "A_x": _A_in["x"],
        "A_k_cache": _A_in["k_cache"],
        "A_v_cache": _A_in["v_cache"],
        "A_wq": _A_in["wq"],
        "A_wo": _A_in["wo"],
        "B_x": _B_in["x"],
        "B_w_gate": _B_in["w_gate"],
        "B_w_up": _B_in["w_up"],
        "B_w_down": _B_in["w_down"],
    }

def reference(A_x, A_k_cache, A_v_cache, A_wq, A_wo, B_x, B_w_gate, B_w_up, B_w_down):
    def _A_ref(x, k_cache, v_cache, wq, wo):
        B, Sq, D = x.shape
        Hq, Dh = 32, 128
        Hkv = k_cache.shape[1]
        rep = Hq // Hkv
        q = (lambda x, *d: x.permute(*d))((x @ wq).reshape(B, Sq, Hq, Dh), 0, 2, 1, 3)
        k = (lambda x, n, axis: torch.repeat_interleave(x, n, dim=axis))(k_cache, rep, axis=1)
        v = (lambda x, n, axis: torch.repeat_interleave(x, n, dim=axis))(v_cache, rep, axis=1)
        scores = torch.einsum("bhqd,bhkd->bhqk", q, k) * (Dh ** -0.5)
        p = _softmax(scores, axis=-1)
        o = torch.einsum("bhqk,bhkd->bhqd", p, v)
        return (lambda x, *d: x.permute(*d))(o, 0, 2, 1, 3).reshape(B, Sq, Hq * Dh) @ wo

    def _B_ref(x, w_gate, w_up, w_down):
        g = torch.einsum("bsd,df->bsf", x, w_gate)
        u = torch.einsum("bsd,df->bsf", x, w_up)
        h = _gelu(g) * u
        return torch.einsum("bsf,fd->bsd", h, w_down)

    x = A_x
    xn = _layernorm(x, axis=-1)
    return x + _A_ref(xn, A_k_cache, A_v_cache, A_wq, A_wo) + _B_ref(xn, B_w_gate, B_w_up, B_w_down)
```

```cpp
#include <hip/hip_runtime.h>
#include <hip/hip_bf16.h>
#include <stdint.h>

namespace {

typedef unsigned v4u __attribute__((ext_vector_type(4)));
typedef unsigned v2u __attribute__((ext_vector_type(2)));
typedef float f32x4 __attribute__((ext_vector_type(4)));
typedef float f32x2 __attribute__((ext_vector_type(2)));
typedef short bf16x8 __attribute__((ext_vector_type(8)));
typedef __amdgpu_buffer_rsrc_t rsrc_t;
#define LAS __attribute__((address_space(3)))
#define GAS __attribute__((address_space(1)))
#define DI __device__ __forceinline__

constexpr int AUX_NT = 2, AUX_SC1 = 16, RSRC_W3 = 0x00020000;
constexpr float C2 = 0.08838834764831845f * 1.4426950408889634f;

constexpr int QP_OFF = 4096;
constexpr int G_OFF  = QP_OFF + 2 * 8 * 1024 * 16;
constexpr int U_OFF  = G_OFF + 8 * 896 * 16;
constexpr int PV_OFF = U_OFF + 8 * 896 * 16;
constexpr int ML_OFF = PV_OFF + 32 * 64 * 32 * 16;
constexpr int FO_OFF = ML_OFF + 32 * 64 * 16;
constexpr int WS_END = FO_OFF + 8 * 8 * 4 * 32 * 16 * 16;

constexpr int XN_STRIDE = 2064;
constexpr int L_XN = 0;
constexpr int L_RED = L_XN + 8 * XN_STRIDE;
constexpr int L_RE2 = L_RED + 34816;
constexpr int L_ML = L_RE2 + 33792;
constexpr int L_QB = L_ML + 512;
constexpr int L_HB = L_QB + 8 * 272;
constexpr int L_OB = L_HB + 8 * 272;
constexpr int L_FR = L_OB + 8 * 272;
constexpr int L_FL = L_FR + 1024;
constexpr int L_END = L_FL + 256;

constexpr unsigned SPIN_CAP = 1u << 18;

enum { R_Q, R_SQ, R_F, R_SF, R_N };
#define KS_Q0 160
#define KU_Q0 120
#define KS_F0 152
#define KU_F0 80
#define KS_SF 120
#define KU_SF 64
#define KS_Q1 158
#define KU_Q1 118
#define KS_SQ 156
#define KU_SQ 112
#define KS_F1 152
#define KU_F1 76
#define KS_N 158
#define KU_N 158
constexpr int C_Q0 = 4 * (KS_Q0 + KU_Q0), C_F0 = 4 * (KS_F0 + KU_F0), C_SF = 4 * (KS_SF + KU_SF);
constexpr int C_Q1 = 4 * (KS_Q1 + KU_Q1), C_SQ = 4 * (KS_SQ + KU_SQ), C_F1 = 4 * (KS_F1 + KU_F1), C_N = 4 * (KS_N + KU_N);
static_assert(5 * C_Q0 + C_SF + 2 * C_F0 == 8192 && C_SQ + 2 * C_Q1 + 4 * C_F1 + C_N == 8192, "the splits of a (batch, kv head) cover its keys");
static_assert(KS_Q0 <= 160 && KS_Q1 <= 160 && KS_N <= 160 && KU_N <= 160 && KU_SF >= 64 && KU_F0 >= 64 && KU_F1 >= 64 && KS_SF >= 64, "at most 5 tiles, the first two whole");

DI f32x4 mfma(v4u a, v4u b, f32x4 c) {
  return __builtin_amdgcn_mfma_f32_16x16x32_bf16(__builtin_bit_cast(bf16x8, a), __builtin_bit_cast(bf16x8, b), c, 0, 0, 0);
}
DI unsigned pk_bf16(float lo, float hi) {
  typedef __bf16 bf2 __attribute__((ext_vector_type(2)));
  typedef float f2 __attribute__((ext_vector_type(2)));
  f2 v = {lo, hi};
  return __builtin_bit_cast(unsigned, __builtin_convertvector(v, bf2));
}
DI float bf_lo(unsigned u) { return __builtin_bit_cast(float, u << 16); }
DI float bf_hi(unsigned u) { return __builtin_bit_cast(float, u & 0xffff0000u); }
DI float rbf(float x) { return bf_lo(pk_bf16(x, x)); }
DI unsigned fu(float x) { return __builtin_bit_cast(unsigned, x); }
DI float uf(unsigned x) { return __builtin_bit_cast(float, x); }
template <int CTRL> DI float dpp(float x) { return uf(__builtin_amdgcn_mov_dpp(fu(x), CTRL, 0xf, 0xf, true)); }
constexpr int ROR8 = 0x128, XOR1 = 0xB1, XOR2 = 0x4E, HMIRR = 0x141;
DI float xrow_max(float x) {
  auto s = __builtin_amdgcn_permlane16_swap(fu(x), fu(x), false, false);
  x = fmaxf(uf(s[0]), uf(s[1]));
  auto t = __builtin_amdgcn_permlane32_swap(fu(x), fu(x), false, false);
  return fmaxf(uf(t[0]), uf(t[1]));
}
DI float xrow_sum(float x) {
  auto s = __builtin_amdgcn_permlane16_swap(fu(x), fu(x), false, false);
  x = uf(s[0]) + uf(s[1]);
  auto t = __builtin_amdgcn_permlane32_swap(fu(x), fu(x), false, false);
  return uf(t[0]) + uf(t[1]);
}
DI float half_sum(float x) {
  auto t = __builtin_amdgcn_permlane32_swap(fu(x), fu(x), false, false);
  return uf(t[0]) + uf(t[1]);
}
DI float row16_sum(float x) {
  x += dpp<XOR1>(x); x += dpp<XOR2>(x); x += dpp<HMIRR>(x); x += dpp<ROR8>(x); return x;
}
DI float wave_sum(float x) { return xrow_sum(row16_sum(x)); }
DI void wg_barrier() {
  asm volatile("s_waitcnt lgkmcnt(0)" ::: "memory");
  __builtin_amdgcn_s_barrier();
  asm volatile("" ::: "memory");
}
DI rsrc_t mk_rsrc(const void* p, unsigned bytes) { return __builtin_amdgcn_make_buffer_rsrc((void*)p, 0, bytes, RSRC_W3); }
DI v4u ld_nt(rsrc_t r, int voff, int soff) { return __builtin_amdgcn_raw_buffer_load_b128(r, voff, soff, AUX_NT); }
DI v4u ld_sc1(rsrc_t r, int voff) { asm volatile("" : "+v"(voff) :: "memory"); return __builtin_amdgcn_raw_buffer_load_b128(r, voff, 0, AUX_SC1); }
DI void st_sc1(rsrc_t r, int voff, v4u v) { __builtin_amdgcn_raw_buffer_store_b128(v, r, voff, 0, AUX_SC1); }
#define SB() __builtin_amdgcn_sched_barrier(0)
constexpr unsigned SPIN_LDS = 1u << 24;
#define SPIN_GUARD0(spins) { asm volatile("" ::: "memory"); if (++(spins) > SPIN_CAP) break; }
#define SPIN_GUARD(spins) { __builtin_amdgcn_s_sleep(2); asm volatile("" ::: "memory"); if (++(spins) > SPIN_CAP) break; }

DI void flag_set(LAS unsigned char* L, int phase, int w, int lane, unsigned T0, unsigned T1) {
  asm volatile("s_waitcnt lgkmcnt(0)" ::: "memory");
  if (lane == 0) *(volatile LAS v2u*)(L + L_FL + (phase * 4 + w) * 8) = v2u{T0, T1};
}
DI void flag_wait(LAS unsigned char* L, int phase, int lane, unsigned T0, unsigned T1) {
#pragma nounroll
  for (unsigned spins = 0; spins < SPIN_LDS; ++spins) {
    const v2u f = *(volatile LAS v2u*)(L + L_FL + (phase * 4 + (lane & 3)) * 8);
    if (__all(f.x == T0 && f.y == T1)) break;
    __builtin_amdgcn_s_sleep(1);
  }
  asm volatile("" ::: "memory");
}

DI void ld8a(v4u (&r)[8], rsrc_t rs, int v0, int v1) {
#pragma unroll
  for (int i = 0; i < 4; ++i) r[i] = ld_nt(rs, v0 + (i & 1) * 2048, (i >> 1) * 4096);
#pragma unroll
  for (int i = 0; i < 4; ++i) r[4 + i] = ld_nt(rs, v1 + (i & 1) * 2048, (i >> 1) * 4096);
  SB();
}
DI void ld8v(v4u (&r)[8], rsrc_t rs, int v0) {
#pragma unroll
  for (int i = 0; i < 4; ++i) r[i] = ld_nt(rs, v0 + i * 256, 0);
#pragma unroll
  for (int i = 0; i < 4; ++i) r[4 + i] = ld_nt(rs, v0 + i * 256, 4096);
  SB();
}
DI void ld8u(v4u (&r)[8], rsrc_t rs, int v0, int stride) {
#pragma unroll
  for (int i = 0; i < 8; ++i) r[i] = ld_nt(rs, v0, i * stride);
  SB();
}
DI void tmul(const v4u (&r)[8], v4u bop, f32x4 (&acc)[8]) {
#pragma unroll
  for (int j = 0; j < 8; ++j) {
    const unsigned sel = (j & 1) ? 0x07060302u : 0x05040100u;
    v4u a;
    a.x = __builtin_amdgcn_perm(r[1][j >> 1], r[0][j >> 1], sel);
    a.y = __builtin_amdgcn_perm(r[3][j >> 1], r[2][j >> 1], sel);
    a.z = __builtin_amdgcn_perm(r[5][j >> 1], r[4][j >> 1], sel);
    a.w = __builtin_amdgcn_perm(r[7][j >> 1], r[6][j >> 1], sel);
    acc[j] = mfma(a, bop, acc[j]);
  }
}

template <bool MASK, bool RK, bool RV>
DI void step(v4u (&kr)[8], v4u (&vr)[8], const v4u (&qf)[2], f32x4 (&acc)[8], float& m, float& l, int limit,
             rsrc_t rk, int vk0, int vk1, rsrc_t rv, int vv) {
  f32x4 d[4];
#pragma unroll
  for (int g = 0; g < 4; ++g) {
    d[g] = mfma(kr[g], qf[0], f32x4{0.f, 0.f, 0.f, 0.f});
    d[g] = mfma(kr[4 + g], qf[1], d[g]);
  }
  SB();
  if (RK) ld8a(kr, rk, vk0, vk1);
  float s[8];
#pragma unroll
  for (int sub = 0; sub < 2; ++sub)
#pragma unroll
    for (int r = 0; r < 4; ++r) {
      auto sw = __builtin_amdgcn_permlane32_swap(fu(d[2 * sub][r]), fu(d[2 * sub + 1][r]), false, false);
      float v = uf(sw[0]) + dpp<ROR8>(uf(sw[1]));
      if (MASK) v = (16 * sub + r < limit) ? v : -INFINITY;
      s[4 * sub + r] = v;
    }
  float tm = fmaxf(fmaxf(fmaxf(s[0], s[1]), fmaxf(s[2], s[3])), fmaxf(fmaxf(s[4], s[5]), fmaxf(s[6], s[7])));
  tm = xrow_max(tm);
  const float mn = fmaxf(m, tm);
  const float alpha = __builtin_amdgcn_exp2f((m - mn) * C2);
  const float mc = mn * C2;
  float p[8], ps = 0.f;
#pragma unroll
  for (int i = 0; i < 8; ++i) { p[i] = __builtin_amdgcn_exp2f(fmaf(s[i], C2, -mc)); ps += p[i]; }
  l = fmaf(l, alpha, ps);
  m = mn;
  v4u pb = {pk_bf16(p[0], p[1]), pk_bf16(p[2], p[3]), pk_bf16(p[4], p[5]), pk_bf16(p[6], p[7])};
#pragma unroll
  for (int j = 0; j < 8; ++j) acc[j] *= alpha;
  tmul(vr, pb, acc);
  SB();
  if (RV) ld8v(vr, rv, vv);
}

#define STAMP(i)

__global__ void __launch_bounds__(512, 2)
fused_decode(const unsigned short* __restrict__ X, const unsigned char* __restrict__ KC, const unsigned char* __restrict__ VC,
             const unsigned char* __restrict__ WQ, const unsigned char* __restrict__ WO, const unsigned char* __restrict__ WG,
             const unsigned char* __restrict__ WU, const unsigned char* __restrict__ WD, unsigned short* __restrict__ OUT,
             unsigned char* WS) {
  __shared__ __attribute__((aligned(16))) unsigned char smem[L_END];
  LAS unsigned char* L = (LAS unsigned char*)smem;
  const int tid = threadIdx.x, lane = tid & 63, w = __builtin_amdgcn_readfirstlane(tid >> 6);
  const int n16 = lane & 15, g4 = lane >> 4;
  const int B = blockIdx.x, x = B & 7, j = B >> 3, hk = j >> 3, s = j & 7;
  STAMP(0);
  const unsigned long long ep = *(const unsigned long long*)WS;
  const unsigned T0 = (unsigned)ep + 0x9E3779B9u, T1 = (unsigned)(ep >> 32) + 0x7F4A7C15u;
  const rsrc_t rws = mk_rsrc(WS, WS_END);

  const bool lowg = hk < 2;
  const int role = lowg ? (s < 5 ? R_Q : s == 5 ? R_SF : R_F) : (s == 0 ? R_SQ : s < 3 ? R_Q : s < 7 ? R_F : R_N);
  const bool isQ = role == R_Q || role == R_SQ, none = role == R_N;
  const bool uw = w < 4;
  const int KS = role == R_Q ? (lowg ? KS_Q0 : KS_Q1) : role == R_SQ ? KS_SQ : role == R_F ? (lowg ? KS_F0 : KS_F1) : role == R_SF ? KS_SF : KS_N;
  const int KU = role == R_Q ? (lowg ? KU_Q0 : KU_Q1) : role == R_SQ ? KU_SQ : role == R_F ? (lowg ? KU_F0 : KU_F1) : role == R_SF ? KU_SF : KU_N;
  const int cstart = lowg ? (s <= 5 ? s * C_Q0 : 5 * C_Q0 + C_SF + (s - 6) * C_F0)
                          : (s == 0 ? 0 : s <= 3 ? C_SQ + (s - 1) * C_Q1 : C_SQ + 2 * C_Q1 + (s - 3) * C_F1);
  const int KW = uw ? KU : KS;
  const int kstart = cstart + (uw ? w * KU : 4 * KU + (w - 4) * KS);
  const int nt = (KW + 31) >> 5;
  const size_t kvbase = ((size_t)(x * 4 + hk) * 8192 + (size_t)kstart) * 256;
  const rsrc_t rK = mk_rsrc(KC + kvbase, KW * 256), rV = mk_rsrc(VC + kvbase, KW * 256);
  const int vk = (lane & 7) * 256 + ((lane >> 3) & 1) * 64 + g4 * 16;
  const int vv = g4 * 1024 + n16 * 16;

  v4u ak[8], av[8], bk[8], bv[8];
  f32x4 acc[8];

  if (uw) {
    __builtin_amdgcn_s_setprio(3);
    if (!none) {
      int strip, kh = 0; bool isUp = false;
      if (isQ) {
        const int qi = lowg ? hk * 5 + s : 10 + (hk - 2) * 2 + (s - 1);
        const int sl = role == R_SQ ? 3 : (qi >> 1) + ((qi >> 1) >= 3);
        kh = role == R_SQ ? hk - 2 : qi & 1;
        strip = 8 * x + sl;
      } else {
        const int slow = ((3 + x) & 7) < 6 ? ((3 + x) & 7) : 6;
        const int fi = lowg ? hk * 2 + (s - 6) : 4 + (hk - 2) * 4 + (s - 3);
        const int ni = fi < 6 ? fi : fi - 6;
        isUp = role == R_SF ? hk == 1 : fi >= 6;
        strip = 7 * x + (role == R_SF ? (((3 + x) & 7) < 7 ? ((3 + x) & 7) : 6) : ni + (ni >= (((3 + x) & 7) < 7 ? ((3 + x) & 7) : 6)));
        (void)slow;
      }
      const int stride = isQ ? 8192 : 7168;
      const int k0 = isQ ? 512 * kh + 128 * w : 256 * w;
      const unsigned char* wp = (isQ ? WQ : isUp ? WU : WG) + (size_t)k0 * stride + strip * 128;
      const rsrc_t rU0 = mk_rsrc(wp, 128 * stride), rU1 = mk_rsrc(wp + 128 * (size_t)stride, isQ ? 0 : 128 * stride);
      const GAS v4u* xp = (const GAS v4u*)(X + 2 * w * 1024);
      v4u xr[4];
#pragma unroll
      for (int i = 0; i < 4; ++i) xr[i] = xp[64 * i + lane];
      const int vu = (lane >> 3) * 8 * stride + (lane & 7) * 16;
      SB();
      ld8u(ak, rU0, vu, stride);
      ld8u(av, rU0, vu + 64 * stride, stride);
      if (!isQ) { ld8u(bk, rU1, vu, stride); ld8u(bv, rU1, vu + 64 * stride, stride); }
      flag_set(L, 3, w, lane, T0, T1);
#pragma unroll
      for (int rr = 0; rr < 2; ++rr) {
        const v4u x0 = xr[2 * rr], x1 = xr[2 * rr + 1];
        float xv[16];
#pragma unroll
        for (int i = 0; i < 4; ++i) { xv[2 * i] = bf_lo(x0[i]); xv[2 * i + 1] = bf_hi(x0[i]); xv[8 + 2 * i] = bf_lo(x1[i]); xv[8 + 2 * i + 1] = bf_hi(x1[i]); }
        float sum = 0.f;
#pragma unroll
        for (int i = 0; i < 16; ++i) sum += xv[i];
        const float mean = wave_sum(sum) * (1.f / 1024.f);
        float sq = 0.f;
#pragma unroll
        for (int i = 0; i < 16; ++i) { const float d = xv[i] - mean; sq = fmaf(d, d, sq); }
        const float var = wave_sum(sq) * (1.f / 1024.f);
        const float mb = rbf(mean), sd = rbf(sqrtf(rbf(rbf(var) + 1e-5f)));
        v4u n0, n1;
#pragma unroll
        for (int i = 0; i < 4; ++i) {
          n0[i] = pk_bf16(rbf(xv[2 * i] - mb) / sd, rbf(xv[2 * i + 1] - mb) / sd);
          n1[i] = pk_bf16(rbf(xv[8 + 2 * i] - mb) / sd, rbf(xv[8 + 2 * i + 1] - mb) / sd);
        }
        *(LAS v4u*)(L + L_XN + (2 * w + rr) * XN_STRIDE + lane * 16) = n0;
        *(LAS v4u*)(L + L_XN + (2 * w + rr) * XN_STRIDE + 1024 + lane * 16) = n1;
      }
      flag_set(L, 0, w, lane, T0, T1);
      flag_wait(L, 0, lane, T0, T1);
      STAMP(1);
      const LAS unsigned char* xb = L + L_XN + (n16 & 7) * XN_STRIDE + (k0 + 16 * g4 + 8 * (n16 >> 3)) * 2;
#pragma unroll
      for (int i = 0; i < 8; ++i) acc[i] = f32x4{0.f, 0.f, 0.f, 0.f};
      tmul(ak, *(const LAS v4u*)xb, acc);
      tmul(av, *(const LAS v4u*)(xb + 128), acc);
      if (!isQ) { tmul(bk, *(const LAS v4u*)(xb + 256), acc); tmul(bv, *(const LAS v4u*)(xb + 384), acc); }
      if ((n16 >> 3) == (g4 >> 1)) {
        LAS float* red = (LAS float*)(L + L_RED) + ((w * 2 + (g4 >> 1)) * 8 + (n16 & 7)) * 68 + 32 * (g4 & 1);
#pragma unroll
        for (int r = 0; r < 4; ++r) {
          *(LAS f32x4*)(red + 8 * r) = f32x4{acc[0][r], acc[1][r], acc[2][r], acc[3][r]};
          *(LAS f32x4*)(red + 8 * r + 4) = f32x4{acc[4][r], acc[5][r], acc[6][r], acc[7][r]};
        }
      }
      flag_set(L, 1, w, lane, T0, T1);
      flag_wait(L, 1, lane, T0, T1);
      if (lane < 32) {
        const int b = 2 * w + (lane >> 4), c4 = lane & 15;
        f32x4 t = {0.f, 0.f, 0.f, 0.f};
#pragma unroll
        for (int i = 0; i < 8; ++i) t += *(const LAS f32x4*)((LAS float*)(L + L_RED) + (i * 8 + b) * 68 + 4 * c4);
        const int off = isQ ? QP_OFF + ((kh * 8 + b) * 1024 + strip * 16 + c4) * 16 : (isUp ? U_OFF : G_OFF) + (b * 896 + strip * 16 + c4) * 16;
        st_sc1(rws, off, v4u{pk_bf16(t[0], t[1]), T0, pk_bf16(t[2], t[3]), T1});
      }
      STAMP(2);
      SB();
    }
    ld8a(bk, rK, vk, vk + 128);
    ld8v(bv, rV, vv);
    ld8a(ak, rK, vk + 8192, vk + 8192 + 128);
    ld8v(av, rV, vv + 8192);
    {
      const int qoff = QP_OFF + (((lane >> 5) * 8 + x) * 1024 + 256 * hk + 64 * w + (lane & 31)) * 16;
      v4u v0, v1;
#pragma nounroll
      for (unsigned spins = 0;;) {
        v0 = ld_sc1(rws, qoff); v1 = ld_sc1(rws, qoff + 512);
        if (__all(v0.y == T0 && v0.w == T1 && v1.y == T0 && v1.w == T1)) break;
        SPIN_GUARD(spins);
      }
      const float q0 = half_sum(bf_lo(v0.x)), q1 = half_sum(bf_hi(v0.x)), q2 = half_sum(bf_lo(v0.z)), q3 = half_sum(bf_hi(v0.z));
      const float q4 = half_sum(bf_lo(v1.x)), q5 = half_sum(bf_hi(v1.x)), q6 = half_sum(bf_lo(v1.z)), q7 = half_sum(bf_hi(v1.z));
      if (lane < 32) {
        *(LAS v2u*)(L + L_QB + (2 * w) * 272 + lane * 8) = v2u{pk_bf16(q0, q1), pk_bf16(q2, q3)};
        *(LAS v2u*)(L + L_QB + (2 * w + 1) * 272 + lane * 8) = v2u{pk_bf16(q4, q5), pk_bf16(q6, q7)};
      }
      flag_set(L, 2, w, lane, T0, T1);
    }
    __builtin_amdgcn_s_setprio(0);
    SB();
  } else {
    if (!none) flag_wait(L, 3, lane, T0, T1);
    ld8a(bk, rK, vk, vk + 128);
    ld8v(bv, rV, vv);
    ld8a(ak, rK, vk + 8192, vk + 8192 + 128);
    ld8v(av, rV, vv + 8192);
  }
  flag_wait(L, 2, lane, T0, T1);
  STAMP(3);
  v4u qf[2];
  {
    const LAS unsigned char* qb = L + L_QB + (n16 & 7) * 272 + ((n16 >> 3) * 32 + 8 * g4) * 2;
    qf[0] = *(const LAS v4u*)qb; qf[1] = *(const LAS v4u*)(qb + 128);
  }
#pragma unroll
  for (int i = 0; i < 8; ++i) acc[i] = f32x4{0.f, 0.f, 0.f, 0.f};
  float m = -INFINITY, l = 0.f;
  const int hh = 4 * x + hk, cblk = s;
  const unsigned char* wfp = (w < 4 ? WO : WD) + ((size_t)(128 * hh + 32 * (w & 3)) * 1024 + 128 * cblk) * 2;
  const unsigned wfn = (w >= 4 && hh >= 28) ? 0u : 32u * 2048u;
  const int vw = g4 * 8 * 2048 + n16 * 16;
  const int lim = KW - 4 * g4;
  const rsrc_t rW = mk_rsrc(wfp, wfn);
  step<false, true, true>(bk, bv, qf, acc, m, l, 0, rK, vk + 2 * 8192, vk + 2 * 8192 + 128, rV, vv + 2 * 8192);
  STAMP(4);
  step<false, true, true>(ak, av, qf, acc, m, l, 0, rK, vk + 3 * 8192, vk + 3 * 8192 + 128, rV, vv + 3 * 8192);
  STAMP(5);
  step<true, true, true>(bk, bv, qf, acc, m, l, lim - 64, rK, vk + 4 * 8192, vk + 4 * 8192 + 128, rV, vv + 4 * 8192);
  if (!uw) flag_set(L, 4, w - 4, lane, T0, T1);
  STAMP(6);
  if (nt >= 4) step<true, true, false>(ak, av, qf, acc, m, l, lim - 96, mk_rsrc(wfp, uw ? 0u : wfn), vw, vw + 4 * 2048, rV, 0);
  STAMP(7);
  if (nt >= 5) step<true, false, false>(bk, bv, qf, acc, m, l, lim - 128, rK, 0, 0, rV, 0);
  STAMP(8);
  if (uw) {
    v2u hv[2] = {{0u, 0u}, {0u, 0u}};
    if (hh < 28) {
      const int off = ((lane >> 5) ? U_OFF : G_OFF) + (2 * w * 896 + 32 * hh + (lane & 31)) * 16;
      v4u v[2];
#pragma nounroll
      for (unsigned spins = 0;;) {
        v[0] = ld_sc1(rws, off); v[1] = ld_sc1(rws, off + 896 * 16);
        if (__all(v[0].y == T0 && v[0].w == T1 && v[1].y == T0 && v[1].w == T1)) break;
        SPIN_GUARD(spins);
      }
#pragma unroll
      for (int bb = 0; bb < 2; ++bb) {
        auto s0 = __builtin_amdgcn_permlane32_swap(v[bb].x, v[bb].x, false, false);
        auto s1 = __builtin_amdgcn_permlane32_swap(v[bb].z, v[bb].z, false, false);
        float gq[4] = {bf_lo(s0[0]), bf_hi(s0[0]), bf_lo(s1[0]), bf_hi(s1[0])};
        float uq[4] = {bf_lo(s0[1]), bf_hi(s0[1]), bf_lo(s1[1]), bf_hi(s1[1])};
        float hq_[4];
#pragma unroll
        for (int i = 0; i < 4; ++i) {
          const float gg = gq[i], y = 0.7978845608028654f * (gg + 0.044715f * gg * gg * gg);
          const float th = 1.f - 2.f / (__expf(2.f * y) + 1.f);
          hq_[i] = rbf(0.5f * gg * (1.f + th)) * uq[i];
        }
        hv[bb] = v2u{pk_bf16(hq_[0], hq_[1]), pk_bf16(hq_[2], hq_[3])};
      }
    }
    if (lane < 32) {
      *(LAS v2u*)(L + L_HB + (2 * w) * 272 + lane * 8) = hv[0];
      *(LAS v2u*)(L + L_HB + (2 * w + 1) * 272 + lane * 8) = hv[1];
    }
    flag_wait(L, 4, lane, T0, T1);
    ld8a(ak, rW, vw, vw + 4 * 2048);
  }

  l = xrow_sum(l);
  if (n16 < 8) {
    LAS float* ob = (LAS float*)(L + L_RED) + (w * 8 + n16) * 132 + 32 * g4;
#pragma unroll
    for (int r = 0; r < 4; ++r) {
      *(LAS f32x4*)(ob + 8 * r) = f32x4{acc[0][r], acc[1][r], acc[2][r], acc[3][r]};
      *(LAS f32x4*)(ob + 8 * r + 4) = f32x4{acc[4][r], acc[5][r], acc[6][r], acc[7][r]};
    }
    if (g4 == 0) *(LAS f32x2*)(L + L_ML + (w * 8 + n16) * 8) = f32x2{m, l};
  }
  wg_barrier();
  {
    float mw[8], lw[8], M = -INFINITY;
#pragma unroll
    for (int i = 0; i < 8; ++i) { const f32x2 t = *(const LAS f32x2*)(L + L_ML + (i * 8 + w) * 8); mw[i] = t[0]; lw[i] = t[1]; M = fmaxf(M, t[0]); }
    float Ls = 0.f; f32x4 o = {0.f, 0.f, 0.f, 0.f};
#pragma unroll
    for (int i = 0; i < 8; ++i) {
      const float sc = __builtin_amdgcn_exp2f((mw[i] - M) * C2);
      Ls = fmaf(lw[i], sc, Ls);
      o += sc * *(const LAS f32x4*)((LAS float*)(L + L_RED) + (i * 8 + w) * 132 + 4 * (lane & 31));
    }
    const int head = 8 * hk + w, slot = (head * 8 + x) * 8 + s;
    if (lane <= 32) {
      const bool isml = lane == 32;
      const int off = isml ? ML_OFF + slot * 16 : PV_OFF + (slot * 32 + lane) * 16;
      const v4u gv = isml ? v4u{fu(M * C2), T0, fu(Ls), T1} : v4u{pk_bf16(o[0], o[1]), T0, pk_bf16(o[2], o[3]), T1};
      st_sc1(rws, off, gv);
    }
  }
  STAMP(9);
  v4u at[8];
#pragma unroll
  for (int jj = 0; jj < 8; ++jj) {
    const unsigned sel = (jj & 1) ? 0x07060302u : 0x05040100u;
    at[jj].x = __builtin_amdgcn_perm(ak[1][jj >> 1], ak[0][jj >> 1], sel);
    at[jj].y = __builtin_amdgcn_perm(ak[3][jj >> 1], ak[2][jj >> 1], sel);
    at[jj].z = __builtin_amdgcn_perm(ak[5][jj >> 1], ak[4][jj >> 1], sel);
    at[jj].w = __builtin_amdgcn_perm(ak[7][jj >> 1], ak[6][jj >> 1], sel);
  }
#define OUTPART(LB) do { \
    const v4u bop = *(const LAS v4u*)(L + (LB) + (n16 & 7) * 272 + (32 * (w & 3) + 8 * g4) * 2); \
    _Pragma("unroll") for (int i = 0; i < 8; ++i) acc[i] = mfma(at[i], bop, f32x4{0.f, 0.f, 0.f, 0.f}); \
    if (n16 < 8) { \
      LAS float* ob = (LAS float*)(L + L_RE2) + (w * 8 + n16) * 132 + 32 * g4; \
      _Pragma("unroll") for (int r = 0; r < 4; ++r) { \
        *(LAS f32x4*)(ob + 8 * r) = f32x4{acc[0][r], acc[1][r], acc[2][r], acc[3][r]}; \
        *(LAS f32x4*)(ob + 8 * r + 4) = f32x4{acc[4][r], acc[5][r], acc[6][r], acc[7][r]}; \
      } \
    } } while (0)
  if (!uw) OUTPART(L_HB);
  {
    const int base = PV_OFF + (hh * 8 + w) * 8 * 512 + lane * 16;
    const int mloff = ML_OFF + ((hh * 8 + w) * 8 + (lane & 7)) * 16;
    v4u v[4], ml;
    unsigned want = 0xF, have = 0;
#pragma nounroll
    for (unsigned spins = 0;;) {
      ml = ld_sc1(rws, mloff);
#pragma unroll
      for (int t = 0; t < 4; ++t) if (want & (1u << t)) v[t] = ld_sc1(rws, base + t * 1024);
      const unsigned mlm = (unsigned)__ballot(ml.y == T0 && ml.w == T1) & 0xFFu;
#pragma unroll
      for (int t = 0; t < 4; ++t) if (want & (1u << t)) { if (__all(v[t].y == T0 && v[t].w == T1)) have |= 1u << t; }
      if (mlm == 0xFFu && have == 0xFu) break;
      unsigned ready = 0;
#pragma unroll
      for (int t = 0; t < 4; ++t) if (((mlm >> (2 * t)) & 3u) == 3u) ready |= 1u << t;
      const unsigned miss = ~have & 0xFu;
      want = __builtin_popcount(miss) <= 2 ? miss : (ready & miss);
      SPIN_GUARD0(spins);
    }
    const float ms = uf(ml.x), ls = uf(ml.z);
    float M = ms;
    M = fmaxf(M, dpp<XOR1>(M)); M = fmaxf(M, dpp<XOR2>(M)); M = fmaxf(M, dpp<HMIRR>(M));
    const float e = __builtin_amdgcn_exp2f(ms - M);
    float Ls = e * ls;
    Ls += dpp<XOR1>(Ls); Ls += dpp<XOR2>(Ls); Ls += dpp<HMIRR>(Ls);
    const float wt = e / Ls;
    float o[4] = {0.f, 0.f, 0.f, 0.f};
#pragma unroll
    for (int t = 0; t < 4; ++t) {
      const float wa = uf(__builtin_amdgcn_readlane(fu(wt), 2 * t)), wb = uf(__builtin_amdgcn_readlane(fu(wt), 2 * t + 1));
      const float ww = (lane >> 5) ? wb : wa;
      o[0] = fmaf(ww, bf_lo(v[t].x), o[0]); o[1] = fmaf(ww, bf_hi(v[t].x), o[1]);
      o[2] = fmaf(ww, bf_lo(v[t].z), o[2]); o[3] = fmaf(ww, bf_hi(v[t].z), o[3]);
    }
#pragma unroll
    for (int i = 0; i < 4; ++i) o[i] = half_sum(o[i]);
    if (lane < 32) *(LAS v2u*)(L + L_OB + w * 272 + lane * 8) = v2u{pk_bf16(o[0], o[1]), pk_bf16(o[2], o[3])};
  }
  wg_barrier();
  STAMP(10);
  if (uw) OUTPART(L_OB);
  wg_barrier();
  {
    f32x2 t = {0.f, 0.f};
#pragma unroll
    for (int i = 0; i < 8; ++i) t += *(const LAS f32x2*)((LAS float*)(L + L_RE2) + (i * 8 + w) * 132 + 2 * lane);
    const int off = FO_OFF + ((((cblk * 8 + w) * 4 + (lane >> 4)) * 32 + hh) * 16 + (lane & 15)) * 16;
    st_sc1(rws, off, v4u{fu(t[0]), T0, fu(t[1]), T1});
  }
  STAMP(11);
  if (w == 0) {
    const int off = FO_OFF + (((cblk * 8 + x) * 4 + hk) * 32 * 16 + lane) * 16;
    const int col = 128 * cblk + 32 * hk;
    unsigned xr = 0;
    if (lane < 16) xr = *(const GAS unsigned*)(X + x * 1024 + col + 2 * lane);
    v4u v[8];
#pragma nounroll
    for (unsigned spins = 0;;) {
#pragma unroll
      for (int t = 0; t < 8; ++t) v[t] = ld_sc1(rws, off + t * 1024);
      bool ok = true;
#pragma unroll
      for (int t = 0; t < 8; ++t) ok = ok && v[t].y == T0 && v[t].w == T1;
      if (__all(ok)) break;
      SPIN_GUARD0(spins);
    }
    float a0 = 0.f, a1 = 0.f;
#pragma unroll
    for (int t = 0; t < 8; ++t) { a0 += uf(v[t].x); a1 += uf(v[t].z); }
    a0 = xrow_sum(a0); a1 = xrow_sum(a1);
    if (lane < 16) *(GAS unsigned*)(OUT + x * 1024 + col + 2 * lane) = pk_bf16(bf_lo(xr) + a0, bf_hi(xr) + a1);
    if (B == 0 && lane == 0) *(GAS unsigned long long*)WS = ep + 1;
  }
  STAMP(12);
}

}

extern "C" void kernel_launch(void* const* d_in, const int* in_sizes, int n_in, void* d_out, int out_size, void* d_ws, size_t ws_size,
                              hipStream_t stream) {
  (void)in_sizes; (void)n_in; (void)out_size; (void)ws_size;
  fused_decode<<<256, 512, 0, stream>>>((const unsigned short*)d_in[0], (const unsigned char*)d_in[1], (const unsigned char*)d_in[2],
                                        (const unsigned char*)d_in[3], (const unsigned char*)d_in[4], (const unsigned char*)d_in[6],
                                        (const unsigned char*)d_in[7], (const unsigned char*)d_in[8], (unsigned short*)d_out,
                                        (unsigned char*)d_ws);
}
```

```cpp
#include <hip/hip_runtime.h>
#include <hip/hip_bf16.h>
#include <stdint.h>

namespace {

typedef unsigned v4u __attribute__((ext_vector_type(4)));
typedef unsigned v2u __attribute__((ext_vector_type(2)));
typedef float f32x4 __attribute__((ext_vector_type(4)));
typedef float f32x2 __attribute__((ext_vector_type(2)));
typedef short bf16x8 __attribute__((ext_vector_type(8)));
typedef __amdgpu_buffer_rsrc_t rsrc_t;
#define LAS __attribute__((address_space(3)))
#define GAS __attribute__((address_space(1)))
#define DI __device__ __forceinline__

constexpr int AUX_K = 2, AUX_V = 2, AUX_U = 2, AUX_W = 2;
constexpr int AUX_SC1 = 16, RSRC_W3 = 0x00020000;
constexpr float SCALE = 0.08838834764831845f;
constexpr float C2 = 1.4426950408889634f;

constexpr int QP_OFF = 4096;
constexpr int G_OFF  = QP_OFF + 2 * 8 * 2048 * 16;
constexpr int U_OFF  = G_OFF + 8 * 896 * 16;
constexpr int PV_OFF = U_OFF + 8 * 896 * 16;
constexpr int ML_OFF = PV_OFF + 32 * 64 * 32 * 16;
constexpr int FO_OFF = ML_OFF + 32 * 64 * 16;
constexpr int WS_END = FO_OFF + 8 * 8 * 4 * 32 * 16 * 16;

constexpr int XN_STRIDE = 2064;
constexpr int L_XN = 0;
constexpr int L_RED = L_XN + 8 * XN_STRIDE;
constexpr int L_RE2 = L_RED + 34816;
constexpr int L_ML = L_RE2 + 4 * 8 * 132 * 4;
constexpr int L_QB = L_ML + 512;
constexpr int L_HB = L_QB + 8 * 272;
constexpr int L_OB = L_HB + 8 * 272;
constexpr int L_FL = L_OB + 8 * 272;
constexpr int L_END = L_FL + 256;

constexpr unsigned SPIN_CAP = 1u << 18;

enum { R_Q = 0, R_SQ = 1, R_F = 2, R_SF = 3, R_N = 4 };
#define KS_Q0 160
#define KU_Q0 120
#define KS_F0 160
#define KU_F0 76
#define KS_SF 112
#define KU_SF 64
#define KS_Q1 160
#define KU_Q1 118
#define KS_SQ 148
#define KU_SQ 104
#define KS_F1 154
#define KU_F1 76
#define KS_N 160
#define KU_N 160
constexpr int C_Q0 = 4 * (KS_Q0 + KU_Q0), C_F0 = 4 * (KS_F0 + KU_F0), C_SF = 4 * (KS_SF + KU_SF);
constexpr int C_Q1 = 4 * (KS_Q1 + KU_Q1), C_SQ = 4 * (KS_SQ + KU_SQ), C_F1 = 4 * (KS_F1 + KU_F1), C_N = 4 * (KS_N + KU_N);
static_assert(5 * C_Q0 + C_SF + 2 * C_F0 == 8192 && C_SQ + 2 * C_Q1 + 4 * C_F1 + C_N == 8192, "the splits of a (batch, kv head) cover its keys");
static_assert(KS_Q0 <= 160 && KS_Q1 <= 160 && KS_N <= 160 && KU_N <= 160 && KU_SF >= 64 && KU_F0 >= 64 && KU_F1 >= 64 && KS_SF > 96 && KS_SQ > 96 && KS_F0 > 96 && KS_F1 > 96, "at most 5 tiles of 32 keys per wave, the first two whole");
constexpr int role_of(int g, int s) { return g == 0 ? (s < 5 ? R_Q : s == 5 ? R_SF : R_F) : (s == 0 ? R_SQ : s < 3 ? R_Q : s < 7 ? R_F : R_N); }
constexpr int ks_of(int g, int s) { return role_of(g, s) == R_Q ? (g == 0 ? KS_Q0 : KS_Q1) : role_of(g, s) == R_SQ ? KS_SQ : role_of(g, s) == R_F ? (g == 0 ? KS_F0 : KS_F1) : role_of(g, s) == R_SF ? KS_SF : KS_N; }
constexpr int ku_of(int g, int s) { return role_of(g, s) == R_Q ? (g == 0 ? KU_Q0 : KU_Q1) : role_of(g, s) == R_SQ ? KU_SQ : role_of(g, s) == R_F ? (g == 0 ? KU_F0 : KU_F1) : role_of(g, s) == R_SF ? KU_SF : KU_N; }
constexpr int start_of(int g, int s) { return s == 0 ? 0 : start_of(g, s - 1) + 4 * (ks_of(g, s - 1) + ku_of(g, s - 1)); }
constexpr unsigned ent_of(int g, int s) { return (unsigned)role_of(g, s) | ((unsigned)ks_of(g, s) << 3) | ((unsigned)ku_of(g, s) << 11) | ((unsigned)(start_of(g, s) / 4) << 19); }
constexpr unsigned long long tab_of(int g, int s) { return (unsigned long long)ent_of(g, s) | ((unsigned long long)ent_of(g, s + 1) << 32); }
template <int G, int S> struct Tab { static constexpr unsigned long long v = tab_of(G, S); };
#define TAB(g, s) (Tab<g, s>::v)

DI f32x4 mfma(v4u a, v4u b, f32x4 c) {
  return __builtin_amdgcn_mfma_f32_16x16x32_bf16(__builtin_bit_cast(bf16x8, a), __builtin_bit_cast(bf16x8, b), c, 0, 0, 0);
}
DI unsigned pk_bf16(float lo, float hi) {
  typedef __bf16 bf2 __attribute__((ext_vector_type(2)));
  typedef float f2 __attribute__((ext_vector_type(2)));
  f2 v = {lo, hi};
  return __builtin_bit_cast(unsigned, __builtin_convertvector(v, bf2));
}
DI float bf_lo(unsigned u) { return __builtin_bit_cast(float, u << 16); }
DI float bf_hi(unsigned u) { return __builtin_bit_cast(float, u & 0xffff0000u); }
DI float rbf(float x) { return bf_lo(pk_bf16(x, x)); }
DI unsigned fu(float x) { return __builtin_bit_cast(unsigned, x); }
DI float uf(unsigned x) { return __builtin_bit_cast(float, x); }
template <int CTRL> DI float dpp(float x) { return uf(__builtin_amdgcn_mov_dpp(fu(x), CTRL, 0xf, 0xf, true)); }
constexpr int ROR8 = 0x128, XOR1 = 0xB1, XOR2 = 0x4E, HMIRR = 0x141;
DI float xrow_max(float x) {
  auto s = __builtin_amdgcn_permlane16_swap(fu(x), fu(x), false, false);
  x = fmaxf(uf(s[0]), uf(s[1]));
  auto t = __builtin_amdgcn_permlane32_swap(fu(x), fu(x), false, false);
  return fmaxf(uf(t[0]), uf(t[1]));
}
DI float xrow_sum(float x) {
  auto s = __builtin_amdgcn_permlane16_swap(fu(x), fu(x), false, false);
  x = uf(s[0]) + uf(s[1]);
  auto t = __builtin_amdgcn_permlane32_swap(fu(x), fu(x), false, false);
  return uf(t[0]) + uf(t[1]);
}
DI float half_sum(float x) {
  auto t = __builtin_amdgcn_permlane32_swap(fu(x), fu(x), false, false);
  return uf(t[0]) + uf(t[1]);
}
DI float row16_sum(float x) {
  x += dpp<XOR1>(x); x += dpp<XOR2>(x); x += dpp<HMIRR>(x); x += dpp<ROR8>(x); return x;
}
DI float wave_sum(float x) { return xrow_sum(row16_sum(x)); }
DI void wg_barrier() {
  asm volatile("s_waitcnt lgkmcnt(0)" ::: "memory");
  __builtin_amdgcn_s_barrier();
  asm volatile("" ::: "memory");
}
DI rsrc_t mk_rsrc(const void* p, unsigned bytes) { return __builtin_amdgcn_make_buffer_rsrc((void*)p, 0, bytes, RSRC_W3); }
template <int AUX> DI v4u ld_nt(rsrc_t r, int voff, int soff) { return __builtin_amdgcn_raw_buffer_load_b128(r, voff, soff, AUX); }
DI v4u ld_sc1(rsrc_t r, int voff) { asm volatile("" : "+v"(voff) :: "memory"); return __builtin_amdgcn_raw_buffer_load_b128(r, voff, 0, AUX_SC1); }
DI void st_sc1(rsrc_t r, int voff, v4u v) { __builtin_amdgcn_raw_buffer_store_b128(v, r, voff, 0, AUX_SC1); }
#define SB() __builtin_amdgcn_sched_barrier(0)
constexpr unsigned SPIN_LDS = 1u << 24;
#define SPIN_GUARD0(spins) { asm volatile("" ::: "memory"); if (++(spins) > SPIN_CAP) break; }
#define SPIN_GUARD(spins) { __builtin_amdgcn_s_sleep(2); asm volatile("" ::: "memory"); if (++(spins) > SPIN_CAP) break; }

DI void flag_set(LAS unsigned char* L, int phase, int w, int lane) {
  asm volatile("s_waitcnt lgkmcnt(0)" ::: "memory");
  if (lane == 0) *(volatile LAS unsigned*)(L + L_FL + (phase * 4 + w) * 4) = 1u;
}
DI void flag_wait(LAS unsigned char* L, int phase, int lane) {
#pragma nounroll
  for (unsigned spins = 0; spins < SPIN_LDS; ++spins) {
    const unsigned f = *(volatile LAS unsigned*)(L + L_FL + (phase * 4 + (lane & 3)) * 4);
    if (__all(f == 1u)) break;
    __builtin_amdgcn_s_sleep(1);
  }
  asm volatile("" ::: "memory");
}

template <int AUX = AUX_K> DI void ld8a(v4u (&r)[8], rsrc_t rs, int v0, int v1) {
#pragma unroll
  for (int i = 0; i < 4; ++i) r[i] = ld_nt<AUX>(rs, v0 + (i & 1) * 2048, (i >> 1) * 4096);
#pragma unroll
  for (int i = 0; i < 4; ++i) r[4 + i] = ld_nt<AUX>(rs, v1 + (i & 1) * 2048, (i >> 1) * 4096);
  SB();
}
DI void ld8v(v4u (&r)[8], rsrc_t rs, int v0) {
#pragma unroll
  for (int i = 0; i < 4; ++i) r[i] = ld_nt<AUX_V>(rs, v0 + i * 256, 0);
#pragma unroll
  for (int i = 0; i < 4; ++i) r[4 + i] = ld_nt<AUX_V>(rs, v0 + i * 256, 4096);
  SB();
}
DI void ld8u(v4u (&r)[8], rsrc_t rs, int v0, int stride) {
#pragma unroll
  for (int i = 0; i < 8; ++i) r[i] = ld_nt<AUX_U>(rs, v0, i * stride);
  SB();
}
DI void tmul(const v4u (&r)[8], v4u bop, f32x4 (&acc)[8]) {
#pragma unroll
  for (int j = 0; j < 8; ++j) {
    const unsigned sel = (j & 1) ? 0x07060302u : 0x05040100u;
    v4u a;
    a.x = __builtin_amdgcn_perm(r[1][j >> 1], r[0][j >> 1], sel);
    a.y = __builtin_amdgcn_perm(r[3][j >> 1], r[2][j >> 1], sel);
    a.z = __builtin_amdgcn_perm(r[5][j >> 1], r[4][j >> 1], sel);
    a.w = __builtin_amdgcn_perm(r[7][j >> 1], r[6][j >> 1], sel);
    acc[j] = mfma(a, bop, acc[j]);
  }
}

template <bool MASK, bool RK, bool RV, int AUXK = AUX_K>
DI void step(v4u (&kr)[8], v4u (&vr)[8], const v4u (&qf)[2], f32x4 (&acc)[8], float& m, float& l, int limit,
             rsrc_t rk, int vk0, int vk1, rsrc_t rv, int vv) {
  f32x4 d[4];
#pragma unroll
  for (int g = 0; g < 4; ++g) {
    d[g] = mfma(kr[g], qf[0], f32x4{0.f, 0.f, 0.f, 0.f});
    d[g] = mfma(kr[4 + g], qf[1], d[g]);
  }
  SB();
  if (RK) ld8a<AUXK>(kr, rk, vk0, vk1);
  float s[8];
#pragma unroll
  for (int sub = 0; sub < 2; ++sub)
#pragma unroll
    for (int r = 0; r < 4; ++r) {
      auto sw = __builtin_amdgcn_permlane32_swap(fu(d[2 * sub][r]), fu(d[2 * sub + 1][r]), false, false);
      s[4 * sub + r] = uf(sw[0]) + dpp<ROR8>(uf(sw[1]));
    }
#pragma unroll
  for (int i = 0; i < 8; i += 2) {
    const unsigned a = pk_bf16(s[i], s[i + 1]);
    const unsigned b = pk_bf16(bf_lo(a) * SCALE, bf_hi(a) * SCALE);
    s[i] = bf_lo(b); s[i + 1] = bf_hi(b);
  }
  if (MASK) {
#pragma unroll
    for (int i = 0; i < 8; ++i) s[i] = (16 * (i >> 2) + (i & 3) < limit) ? s[i] : -INFINITY;
  }
  float tm = fmaxf(fmaxf(fmaxf(s[0], s[1]), fmaxf(s[2], s[3])), fmaxf(fmaxf(s[4], s[5]), fmaxf(s[6], s[7])));
  tm = xrow_max(tm);
  const float mn = fmaxf(m, tm);
  const float alpha = __builtin_amdgcn_exp2f((m - mn) * C2);
  const float mc = mn * C2;
  float p[8], ps = 0.f;
#pragma unroll
  for (int i = 0; i < 8; ++i) { p[i] = __builtin_amdgcn_exp2f(fmaf(s[i], C2, -mc)); ps += p[i]; }
  l = fmaf(l, alpha, ps);
  m = mn;
  v4u pb = {pk_bf16(p[0], p[1]), pk_bf16(p[2], p[3]), pk_bf16(p[4], p[5]), pk_bf16(p[6], p[7])};
#pragma unroll
  for (int j = 0; j < 8; ++j) acc[j] *= alpha;
  tmul(vr, pb, acc);
  SB();
  if (RV) ld8v(vr, rv, vv);
}


__global__ void __launch_bounds__(512, 2)
fused_decode(const unsigned short* __restrict__ X, const unsigned char* __restrict__ KC, const unsigned char* __restrict__ VC,
             const unsigned char* __restrict__ WQ, const unsigned char* __restrict__ WO, const unsigned char* __restrict__ WG,
             const unsigned char* __restrict__ WU, const unsigned char* __restrict__ WD, unsigned short* __restrict__ OUT,
             unsigned char* WS) {
  __shared__ __attribute__((aligned(16))) unsigned char smem[L_END];
  LAS unsigned char* L = (LAS unsigned char*)smem;
  asm volatile("" :: "s"(X), "s"(KC), "s"(VC), "s"(WQ), "s"(WO), "s"(WG), "s"(WU), "s"(WD), "s"(OUT), "s"(WS));
  const int tid = threadIdx.x, lane = tid & 63, w = __builtin_amdgcn_readfirstlane(tid >> 6);
  const int n16 = lane & 15, g4 = lane >> 4;
  const int B = blockIdx.x, x = B & 7, j = B >> 3, hk = j >> 3, s = j & 7;
  const bool lowg = hk < 2;
  const unsigned long long ent = lowg ? (s < 4 ? (s < 2 ? TAB(0, 0) : TAB(0, 2)) : (s < 6 ? TAB(0, 4) : TAB(0, 6)))
                                      : (s < 4 ? (s < 2 ? TAB(1, 0) : TAB(1, 2)) : (s < 6 ? TAB(1, 4) : TAB(1, 6)));
  const unsigned e32 = (unsigned)(ent >> ((s & 1) * 32));
  const int role = e32 & 7, KS = (e32 >> 3) & 255, KU = (e32 >> 11) & 255, cstart = (e32 >> 19) * 4;
  const bool isQ = role <= R_SQ, none = role == R_N;
  const bool uw = w < 4;
  const int KW = uw ? KU : KS;
  const int kstart = cstart + (uw ? w * KU : 4 * KU + (w - 4) * KS);
  const int nt = (KW + 31) >> 5;
  if (tid < 64) *(LAS unsigned*)(L + L_FL + tid * 4) = 0u;
  wg_barrier();
  const unsigned long long ep = *(const __attribute__((address_space(4))) unsigned long long*)WS;
  const rsrc_t rws = mk_rsrc(WS, WS_END);
#define TAGS() unsigned long long ep_ = ep; asm volatile("" : "+s"(ep_)); \
               const unsigned T0 = (unsigned)ep_ + 0x9E3779B9u, T1 = (unsigned)(ep_ >> 32) + 0x7F4A7C15u
  const size_t kvbase = ((size_t)(x * 4 + hk) * 8192 + (size_t)kstart) * 256;
  const rsrc_t rK = mk_rsrc(KC + kvbase, KW * 256), rV = mk_rsrc(VC + kvbase, KW * 256);
  const int vk = (lane & 7) * 256 + ((lane >> 3) & 1) * 64 + g4 * 16;
  const int vv = g4 * 1024 + n16 * 16;

  v4u ak[8], av[8], bk[8], bv[8];
  f32x4 acc[8];

  if (uw) {
    __builtin_amdgcn_s_setprio(3);
    if (!none) {
      int strip, kh = 0; bool isUp = false;
      if (isQ) {
        const int qi = lowg ? hk * 5 + s : 10 + (hk - 2) * 2 + (s - 1);
        const int sl = role == R_SQ ? 3 : (qi >> 1) + ((qi >> 1) >= 3);
        kh = role == R_SQ ? hk - 2 : qi & 1;
        strip = 8 * x + sl;
      } else {
        const int slow = ((3 + x) & 7) < 7 ? ((3 + x) & 7) : 6;
        const int fi = lowg ? hk * 2 + (s - 6) : 4 + (hk - 2) * 4 + (s - 3);
        const int ni = fi < 6 ? fi : fi - 6;
        isUp = role == R_SF ? hk == 1 : fi >= 6;
        strip = 7 * x + (role == R_SF ? slow : ni + (ni >= slow));
      }
      const int stride = isQ ? 8192 : 7168;
      const int k0 = isQ ? 512 * kh + 128 * w : 256 * w;
      const unsigned char* wp = (isQ ? WQ : isUp ? WU : WG) + (size_t)k0 * stride + strip * 128;
      const rsrc_t rU0 = mk_rsrc(wp, 128 * stride), rU1 = mk_rsrc(wp + 128 * (size_t)stride, isQ ? 0 : 128 * stride);
      const int vu = (lane >> 3) * 8 * stride + (lane & 7) * 16;
      SB();
      ld8u(ak, rU0, vu, stride);
      ld8u(av, rU0, vu + 64 * stride, stride);
      if (!isQ) { ld8u(bk, rU1, vu, stride); ld8u(bv, rU1, vu + 64 * stride, stride); }
      flag_set(L, 3, w, lane);
      flag_wait(L, 0, lane);
      const LAS unsigned char* xb = L + L_XN + (n16 & 7) * XN_STRIDE + (k0 + 16 * g4 + 8 * (n16 >> 3)) * 2;
#pragma unroll
      for (int i = 0; i < 8; ++i) acc[i] = f32x4{0.f, 0.f, 0.f, 0.f};
      tmul(ak, *(const LAS v4u*)xb, acc);
      tmul(av, *(const LAS v4u*)(xb + 128), acc);
      if (!isQ) { tmul(bk, *(const LAS v4u*)(xb + 256), acc); tmul(bv, *(const LAS v4u*)(xb + 384), acc); }
      if ((n16 >> 3) == (g4 >> 1)) {
        LAS float* red = (LAS float*)(L + L_RED) + ((w * 2 + (g4 >> 1)) * 8 + (n16 & 7)) * 68 + 32 * (g4 & 1);
#pragma unroll
        for (int r = 0; r < 4; ++r) {
          *(LAS f32x4*)(red + 8 * r) = f32x4{acc[0][r], acc[1][r], acc[2][r], acc[3][r]};
          *(LAS f32x4*)(red + 8 * r + 4) = f32x4{acc[4][r], acc[5][r], acc[6][r], acc[7][r]};
        }
      }
      flag_set(L, 1, w, lane);
      flag_wait(L, 1, lane);
      TAGS();
      if (isQ) {
        const int b = 2 * w + (lane >> 5), c2 = lane & 31;
        f32x2 t = {0.f, 0.f};
#pragma unroll
        for (int i = 0; i < 8; ++i) t += *(const LAS f32x2*)((LAS float*)(L + L_RED) + (i * 8 + b) * 68 + 2 * c2);
        st_sc1(rws, QP_OFF + ((kh * 8 + b) * 2048 + strip * 32 + c2) * 16, v4u{fu(t[0]), T0, fu(t[1]), T1});
      } else if (lane < 32) {
        const int b = 2 * w + (lane >> 4), c4 = lane & 15;
        f32x4 t = {0.f, 0.f, 0.f, 0.f};
#pragma unroll
        for (int i = 0; i < 8; ++i) t += *(const LAS f32x4*)((LAS float*)(L + L_RED) + (i * 8 + b) * 68 + 4 * c4);
        st_sc1(rws, (isUp ? U_OFF : G_OFF) + (b * 896 + strip * 16 + c4) * 16, v4u{pk_bf16(t[0], t[1]), T0, pk_bf16(t[2], t[3]), T1});
      }
      SB();
    }
    ld8a(bk, rK, vk, vk + 128);
    ld8v(bv, rV, vv);
    {
      TAGS();
      const int qoff = QP_OFF + (x * 2048 + 512 * hk + 128 * w + lane) * 16;
      v4u v[4];
#pragma nounroll
      for (unsigned spins = 0;;) {
        v[0] = ld_sc1(rws, qoff); v[1] = ld_sc1(rws, qoff + 1024);
        v[2] = ld_sc1(rws, qoff + 8 * 2048 * 16); v[3] = ld_sc1(rws, qoff + 8 * 2048 * 16 + 1024);
        bool ok = true;
#pragma unroll
        for (int t = 0; t < 4; ++t) ok = ok && v[t].y == T0 && v[t].w == T1;
        if (__all(ok)) break;
        SPIN_GUARD(spins);
      }
      *(LAS unsigned*)(L + L_QB + (2 * w) * 272 + lane * 4) = pk_bf16(uf(v[0].x) + uf(v[2].x), uf(v[0].z) + uf(v[2].z));
      *(LAS unsigned*)(L + L_QB + (2 * w + 1) * 272 + lane * 4) = pk_bf16(uf(v[1].x) + uf(v[3].x), uf(v[1].z) + uf(v[3].z));
      flag_set(L, 2, w, lane);
    }
    __builtin_amdgcn_s_setprio(0);
    SB();
    ld8a(ak, rK, vk + 8192, vk + 8192 + 128);
    ld8v(av, rV, vv + 8192);
  } else {
    if (!none) {
      const GAS v4u* xp = (const GAS v4u*)(X + 2 * (w - 4) * 1024);
      v4u xr[4];
#pragma unroll
      for (int i = 0; i < 4; ++i) xr[i] = xp[64 * i + lane];
      unsigned warm = 0;
      if (lane < 2) warm = *(const GAS unsigned*)((lane == 0 ? KC : VC) + kvbase);
      SB();
#pragma unroll
      for (int rr = 0; rr < 2; ++rr) {
        const v4u x0 = xr[2 * rr], x1 = xr[2 * rr + 1];
        float xv[16];
#pragma unroll
        for (int i = 0; i < 4; ++i) { xv[2 * i] = bf_lo(x0[i]); xv[2 * i + 1] = bf_hi(x0[i]); xv[8 + 2 * i] = bf_lo(x1[i]); xv[8 + 2 * i + 1] = bf_hi(x1[i]); }
        float sum = 0.f;
#pragma unroll
        for (int i = 0; i < 16; ++i) sum += xv[i];
        const float mean = wave_sum(sum) * (1.f / 1024.f);
        float sq = 0.f;
#pragma unroll
        for (int i = 0; i < 16; ++i) { const float d = xv[i] - mean; sq = fmaf(d, d, sq); }
        const float var = wave_sum(sq) * (1.f / 1024.f);
        const float mb = rbf(mean), sd = rbf(sqrtf(rbf(rbf(var) + 1e-5f)));
        const float rs = 1.f / sd;
        v4u n0, n1;
#pragma unroll
        for (int i = 0; i < 4; ++i) {
          n0[i] = pk_bf16(rbf(xv[2 * i] - mb) * rs, rbf(xv[2 * i + 1] - mb) * rs);
          n1[i] = pk_bf16(rbf(xv[8 + 2 * i] - mb) * rs, rbf(xv[8 + 2 * i + 1] - mb) * rs);
        }
        *(LAS v4u*)(L + L_XN + (2 * (w - 4) + rr) * XN_STRIDE + lane * 16) = n0;
        *(LAS v4u*)(L + L_XN + (2 * (w - 4) + rr) * XN_STRIDE + 1024 + lane * 16) = n1;
      }
      flag_set(L, 0, w - 4, lane);
      asm volatile("" :: "v"(warm));
      flag_wait(L, 3, lane);
    }
    ld8a(bk, rK, vk, vk + 128);
    ld8v(bv, rV, vv);
    ld8a(ak, rK, vk + 8192, vk + 8192 + 128);
    ld8v(av, rV, vv + 8192);
  }
  flag_wait(L, 2, lane);
  v4u qf[2];
  {
    const LAS unsigned char* qb = L + L_QB + (n16 & 7) * 272 + ((n16 >> 3) * 32 + 8 * g4) * 2;
    qf[0] = *(const LAS v4u*)qb; qf[1] = *(const LAS v4u*)(qb + 128);
  }
#pragma unroll
  for (int i = 0; i < 8; ++i) acc[i] = f32x4{0.f, 0.f, 0.f, 0.f};
  float m = -INFINITY, l = 0.f;
  const int hh = 4 * x + hk, cblk = s;
  const size_t wfo = ((size_t)(128 * hh + 32 * (w & 3)) * 1024 + 128 * cblk) * 2;
  const int lim = KW - 4 * g4;
  step<false, true, true>(bk, bv, qf, acc, m, l, 0, rK, vk + 2 * 8192, vk + 2 * 8192 + 128, rV, vv + 2 * 8192);
  step<false, true, true>(ak, av, qf, acc, m, l, 0, rK, vk + 3 * 8192, vk + 3 * 8192 + 128, rV, vv + 3 * 8192);
  step<true, true, true>(bk, bv, qf, acc, m, l, lim - 64, rK, vk + 4 * 8192, vk + 4 * 8192 + 128, rV, vv + 4 * 8192);
  if (!uw) flag_set(L, 4, w - 4, lane);
  if (nt >= 4) step<true, false, false>(ak, av, qf, acc, m, l, lim - 96, rK, 0, 0, rV, 0);
  if (nt >= 5) step<true, false, false>(bk, bv, qf, acc, m, l, lim - 128, rK, 0, 0, rV, 0);
  TAGS();
  l = xrow_sum(l);
  if (n16 < 8) {
    LAS float* ob = (LAS float*)(L + L_RED) + (w * 8 + n16) * 132 + 32 * g4;
#pragma unroll
    for (int r = 0; r < 4; ++r) {
      *(LAS f32x4*)(ob + 8 * r) = f32x4{acc[0][r], acc[1][r], acc[2][r], acc[3][r]};
      *(LAS f32x4*)(ob + 8 * r + 4) = f32x4{acc[4][r], acc[5][r], acc[6][r], acc[7][r]};
    }
    if (g4 == 0) *(LAS f32x2*)(L + L_ML + (w * 8 + n16) * 8) = f32x2{m, l};
  }
  flag_set(L, 5 + (w >> 2), w & 3, lane);
  if (uw) {
    v2u hv[2] = {{0u, 0u}, {0u, 0u}};
    if (hh < 28) {
      const int off = ((lane >> 5) ? U_OFF : G_OFF) + (2 * w * 896 + 32 * hh + (lane & 31)) * 16;
      v4u v[2];
#pragma nounroll
      for (unsigned spins = 0;;) {
        v[0] = ld_sc1(rws, off); v[1] = ld_sc1(rws, off + 896 * 16);
        if (__all(v[0].y == T0 && v[0].w == T1 && v[1].y == T0 && v[1].w == T1)) break;
        SPIN_GUARD(spins);
      }
#pragma unroll
      for (int bb = 0; bb < 2; ++bb) {
        auto s0 = __builtin_amdgcn_permlane32_swap(v[bb].x, v[bb].x, false, false);
        auto s1 = __builtin_amdgcn_permlane32_swap(v[bb].z, v[bb].z, false, false);
        float gq[4] = {bf_lo(s0[0]), bf_hi(s0[0]), bf_lo(s1[0]), bf_hi(s1[0])};
        float uq[4] = {bf_lo(s0[1]), bf_hi(s0[1]), bf_lo(s1[1]), bf_hi(s1[1])};
        float hq_[4];
#pragma unroll
        for (int i = 0; i < 4; ++i) {
          const float gg = gq[i], y = 0.7978845608028654f * (gg + 0.044715f * gg * gg * gg);
          const float th = 1.f - 2.f / (__expf(2.f * y) + 1.f);
          hq_[i] = rbf(0.5f * gg * (1.f + th)) * uq[i];
        }
        hv[bb] = v2u{pk_bf16(hq_[0], hq_[1]), pk_bf16(hq_[2], hq_[3])};
      }
    }
    if (lane < 32) {
      *(LAS v2u*)(L + L_HB + (2 * w) * 272 + lane * 8) = hv[0];
      *(LAS v2u*)(L + L_HB + (2 * w + 1) * 272 + lane * 8) = hv[1];
    }
    flag_set(L, 7, w, lane);
    if (w == 0) {
      unsigned warm = 0;
      if (lane == 0) warm = *(const GAS unsigned*)(WS + WS_END - 16);
      if (lane == 1) warm = *(const GAS unsigned*)((const unsigned char*)OUT + x * 2048);
      asm volatile("" :: "v"(warm));
    }
    int lane2 = lane; asm volatile("" : "+v"(lane2));
    flag_wait(L, 4, lane2);
    const int vw = (lane2 >> 4) * 8 * 2048 + (lane2 & 15) * 16;
    ld8a<AUX_W>(ak, mk_rsrc(WO + wfo, 32u * 2048u), vw, vw + 4 * 2048);
    ld8a<AUX_W>(bk, mk_rsrc(WD + (hh < 28 ? wfo : 0), hh < 28 ? 32u * 2048u : 0u), vw, vw + 4 * 2048);
#pragma unroll
    for (int i = 0; i < 8; ++i) acc[i] = f32x4{0.f, 0.f, 0.f, 0.f};
    v4u at[8];
#pragma unroll
    for (int jj = 0; jj < 8; ++jj) {
      const unsigned sel = (jj & 1) ? 0x07060302u : 0x05040100u;
      at[jj].x = __builtin_amdgcn_perm(ak[1][jj >> 1], ak[0][jj >> 1], sel);
      at[jj].y = __builtin_amdgcn_perm(ak[3][jj >> 1], ak[2][jj >> 1], sel);
      at[jj].z = __builtin_amdgcn_perm(ak[5][jj >> 1], ak[4][jj >> 1], sel);
      at[jj].w = __builtin_amdgcn_perm(ak[7][jj >> 1], ak[6][jj >> 1], sel);
    }
    flag_wait(L, 7, lane);
    tmul(bk, *(const LAS v4u*)(L + L_HB + (n16 & 7) * 272 + (32 * w + 8 * g4) * 2), acc);
    flag_wait(L, 9, lane);
    const v4u bop = *(const LAS v4u*)(L + L_OB + (n16 & 7) * 272 + (32 * w + 8 * g4) * 2);
#pragma unroll
    for (int i = 0; i < 8; ++i) acc[i] = mfma(at[i], bop, acc[i]);
    if (n16 < 8) {
      LAS float* ob = (LAS float*)(L + L_RE2) + (w * 8 + n16) * 132 + 32 * g4;
#pragma unroll
      for (int r = 0; r < 4; ++r) {
        *(LAS f32x4*)(ob + 8 * r) = f32x4{acc[0][r], acc[1][r], acc[2][r], acc[3][r]};
        *(LAS f32x4*)(ob + 8 * r + 4) = f32x4{acc[4][r], acc[5][r], acc[6][r], acc[7][r]};
      }
    }
  } else {
    __builtin_amdgcn_s_setprio(3);
    flag_wait(L, 5, lane);
    flag_wait(L, 6, lane);
    {
    const int hl = 2 * (w - 4) + (lane >> 5);
    float mw[8], lw[8], M = -INFINITY;
#pragma unroll
    for (int i = 0; i < 8; ++i) { const f32x2 t = *(const LAS f32x2*)(L + L_ML + (i * 8 + hl) * 8); mw[i] = t[0]; lw[i] = t[1]; M = fmaxf(M, t[0]); }
    float Ls = 0.f; f32x4 o = {0.f, 0.f, 0.f, 0.f};
#pragma unroll
    for (int i = 0; i < 8; ++i) {
      const float sc = __builtin_amdgcn_exp2f((mw[i] - M) * C2);
      Ls = fmaf(lw[i], sc, Ls);
      o += sc * *(const LAS f32x4*)((LAS float*)(L + L_RED) + (i * 8 + hl) * 132 + 4 * (lane & 31));
    }
    const int slot = ((8 * hk + hl) * 8 + x) * 8 + s;
    st_sc1(rws, PV_OFF + (slot * 32 + (lane & 31)) * 16, v4u{pk_bf16(o[0], o[1]), T0, pk_bf16(o[2], o[3]), T1});
    if ((lane & 31) == 0) st_sc1(rws, ML_OFF + slot * 16, v4u{fu(M * C2), T0, fu(Ls), T1});
    }
    const int b0 = 2 * (w - 4);
    const int base = PV_OFF + (hh * 8 + b0) * 8 * 512 + lane * 16;
    const int mloff = ML_OFF + ((hh * 8 + b0) * 8 + (lane & 15)) * 16;
    v4u v[8], ml;
#pragma unroll
    for (int t = 0; t < 8; ++t) v[t] = v4u{0u, 0u, 0u, 0u};
    unsigned want = 0, have = 0;
#pragma nounroll
    for (unsigned spins = 0;;) {
      ml = ld_sc1(rws, mloff);
#pragma unroll
      for (int t = 0; t < 8; ++t) if (want & (1u << t)) v[t] = ld_sc1(rws, base + t * 1024);
      const unsigned mlm = (unsigned)__ballot(ml.y == T0 && ml.w == T1) & 0xFFFFu;
#pragma unroll
      for (int t = 0; t < 8; ++t) if (want & (1u << t)) { if (__all(v[t].y == T0 && v[t].w == T1)) have |= 1u << t; }
      if (mlm == 0xFFFFu && have == 0xFFu) break;
      const unsigned both = mlm & (mlm >> 1) & 0x5555u;
      unsigned ready = 0;
#pragma unroll
      for (int t = 0; t < 8; ++t) ready |= ((both >> (2 * t)) & 1u) << t;
      const unsigned miss = ~have & 0xFFu;
      want = __builtin_popcount(miss) <= 2 ? miss : (ready & miss);
      SPIN_GUARD0(spins);
    }
    const float ms = uf(ml.x), ls = uf(ml.z);
    float M = ms;
    M = fmaxf(M, dpp<XOR1>(M)); M = fmaxf(M, dpp<XOR2>(M)); M = fmaxf(M, dpp<HMIRR>(M));
    const float e = __builtin_amdgcn_exp2f(ms - M);
    float Ls = e * ls;
    Ls += dpp<XOR1>(Ls); Ls += dpp<XOR2>(Ls); Ls += dpp<HMIRR>(Ls);
    const float wt = e / Ls;
#pragma unroll
    for (int bb = 0; bb < 2; ++bb) {
      float o[4] = {0.f, 0.f, 0.f, 0.f};
#pragma unroll
      for (int t = 0; t < 4; ++t) {
        const float wa = uf(__builtin_amdgcn_readlane(fu(wt), 8 * bb + 2 * t)), wb = uf(__builtin_amdgcn_readlane(fu(wt), 8 * bb + 2 * t + 1));
        const float ww = (lane >> 5) ? wb : wa;
        const v4u vt = v[4 * bb + t];
        o[0] = fmaf(ww, bf_lo(vt.x), o[0]); o[1] = fmaf(ww, bf_hi(vt.x), o[1]);
        o[2] = fmaf(ww, bf_lo(vt.z), o[2]); o[3] = fmaf(ww, bf_hi(vt.z), o[3]);
      }
#pragma unroll
      for (int i = 0; i < 4; ++i) o[i] = half_sum(o[i]);
      if (lane < 32) *(LAS v2u*)(L + L_OB + (b0 + bb) * 272 + lane * 8) = v2u{pk_bf16(o[0], o[1]), pk_bf16(o[2], o[3])};
    }
    flag_set(L, 9, w - 4, lane);
  }
  wg_barrier();
  {
    f32x2 t = {0.f, 0.f};
#pragma unroll
    for (int i = 0; i < 4; ++i) t += *(const LAS f32x2*)((LAS float*)(L + L_RE2) + (i * 8 + w) * 132 + 2 * lane);
    const int off = FO_OFF + ((((cblk * 8 + w) * 4 + (lane >> 4)) * 32 + hh) * 16 + (lane & 15)) * 16;
    st_sc1(rws, off, v4u{fu(t[0]), T0, fu(t[1]), T1});
  }
  if (w == 0) {
    const int off = FO_OFF + (((cblk * 8 + x) * 4 + hk) * 32 * 16 + lane) * 16;
    const int col = 128 * cblk + 32 * hk;
    unsigned xr = 0;
    if (lane < 16) xr = *(const GAS unsigned*)(X + x * 1024 + col + 2 * lane);
    v4u v[8];
#pragma nounroll
    for (unsigned spins = 0;;) {
#pragma unroll
      for (int t = 0; t < 8; ++t) v[t] = ld_sc1(rws, off + t * 1024);
      bool ok = true;
#pragma unroll
      for (int t = 0; t < 8; ++t) ok = ok && v[t].y == T0 && v[t].w == T1;
      if (__all(ok)) break;
      SPIN_GUARD0(spins);
    }
    float a0 = 0.f, a1 = 0.f;
#pragma unroll
    for (int t = 0; t < 8; ++t) { a0 += uf(v[t].x); a1 += uf(v[t].z); }
    a0 = xrow_sum(a0); a1 = xrow_sum(a1);
    if (lane < 16) *(GAS unsigned*)(OUT + x * 1024 + col + 2 * lane) = pk_bf16(bf_lo(xr) + a0, bf_hi(xr) + a1);
    if (B == 0 && lane == 0) *(GAS unsigned long long*)WS = ep + 1;
  }
}

}


extern "C" void kernel_launch(void* const* d_in, const int* in_sizes, int n_in, void* d_out, int out_size, void* d_ws, size_t ws_size,
                              hipStream_t stream) {
  (void)in_sizes; (void)n_in; (void)out_size; (void)ws_size;
  fused_decode<<<256, 512, 0, stream>>>((const unsigned short*)d_in[0], (const unsigned char*)d_in[1], (const unsigned char*)d_in[2],
                                        (const unsigned char*)d_in[3], (const unsigned char*)d_in[4], (const unsigned char*)d_in[6],
                                        (const unsigned char*)d_in[7], (const unsigned char*)d_in[8], (unsigned short*)d_out,
                                        (unsigned char*)d_ws);
}
```
